```python
import torch

def _softmax(x, axis=-1):
    return torch.softmax(x, dim=axis)
def _gelu(x):
    return torch.nn.functional.gelu(x, approximate="tanh")
def _silu(x):
    return torch.nn.functional.silu(x)
def _sigmoid(x):
    return torch.sigmoid(x)
def _layernorm(x, axis=-1, eps=1e-5):
    m = x.mean(dim=axis, keepdim=True)
    v = x.var(dim=axis, keepdim=True, unbiased=False)
    return (x - m) / torch.sqrt(v + eps)
def _scatter_add(tgt, idx, src, axis=0):
    return tgt.clone().index_add_(axis, idx, src)
def _segment_sum(src, seg, n):
    out = torch.zeros((n,) + src.shape[1:], dtype=src.dtype)
    return out.index_add_(0, seg, src)
def _pad(x, pw):
    flat = tuple(v for pair in reversed(pw) for v in pair)
    return torch.nn.functional.pad(x, flat)

kernel_name = 'standalone__self_sup_conv_stem_hubert__speech'

def setup_inputs():

    _w = (lambda a: (lambda t: t.to(torch.bfloat16) if t.is_floating_point() else t)(torch.as_tensor(a)))
    B, T, D, K = 2, 16000, 512, 5
    scale = (1.0 / (K * D)) ** 0.5
    return {
        "x": _w((lambda *s: torch.randn(*s, dtype=torch.bfloat16) * 0.05)(B, T, D)),
        "w": _w((lambda *s: torch.randn(*s, dtype=torch.bfloat16) * 0.05)(K, D, D) * scale),
        "b": _w((lambda *s: torch.randn(*s, dtype=torch.bfloat16) * 0.05)(D) * 0.1),
        "gamma": _w(torch.ones((D,), dtype=float)),
        "beta": _w(torch.zeros((D,), dtype=float)),
    }

def reference(x, w, b, gamma, beta):
    B, T, D = x.shape
    K = w.shape[0]
    s = 2
    L = (T - K) // s + 1
    y = torch.einsum('blc,cd->bld', x[:, 0 : s * L : s, :], w[0])
    for i in range(1, K):
        y = y + torch.einsum('blc,cd->bld', x[:, i : i + s * L : s, :], w[i])
    y = y + b[None, None, :]
    mu = y.mean(1, keepdims=True)
    var = ((y - mu) ** 2).mean(1, keepdims=True)
    yn = (y - mu) / torch.sqrt(var + 1e-5)
    yn = yn * gamma[None, None, :] + beta[None, None, :]
    return _gelu(yn)
```

```cpp
#include <hip/hip_runtime.h>
#include <hip/hip_bf16.h>
#include <stdint.h>

using bf16x8 = __attribute__((ext_vector_type(8))) short;
using f32x4  = __attribute__((ext_vector_type(4))) float;

static constexpr int CB = 2, CT = 16000, CD = 512, CK = 5, CL = 7998;
static constexpr int KTOT = CK * CD;
static constexpr int BM = 128, BN = 128, BK = 64;
static constexpr int MT_PER_B = (CL + BM - 1) / BM;

__device__ __forceinline__ float bf2f(unsigned short u) { return __uint_as_float(((unsigned)u) << 16); }
__device__ __forceinline__ unsigned short f2bf(float f) {
    __hip_bfloat16 h = __float2bfloat16(f);
    return *reinterpret_cast<unsigned short*>(&h);
}

__global__ void k_transpose_w(const unsigned short* __restrict__ w, unsigned short* __restrict__ wt) {
    __shared__ unsigned short tile[64][66];
    int k0 = blockIdx.x * 64, n0 = blockIdx.y * 64;
    int tx = threadIdx.x & 63, ty = threadIdx.x >> 6;
    for (int r = ty; r < 64; r += 4) tile[r][tx] = w[(size_t)(k0 + r) * CD + n0 + tx];
    __syncthreads();
    for (int r = ty; r < 64; r += 4) wt[(size_t)(n0 + r) * KTOT + k0 + tx] = tile[tx][r];
}

__global__ __launch_bounds__(256) void k_gemm(const unsigned short* __restrict__ x, const unsigned short* __restrict__ wt,
                                              const unsigned short* __restrict__ bias, unsigned short* __restrict__ y,
                                              float* __restrict__ part  ) {
    __shared__ __attribute__((aligned(16))) unsigned short sA[BM][BK + 8];
    __shared__ __attribute__((aligned(16))) unsigned short sB[BN][BK + 8];
    __shared__ float red[4][BN];
    __shared__ float smean[BN];
    int mt = blockIdx.x % MT_PER_B, nt = (blockIdx.x / MT_PER_B) % 4, b = blockIdx.x / (MT_PER_B * 4);
    int tid = threadIdx.x, wid = tid >> 6, lane = tid & 63, wr = wid >> 1, wc = wid & 1, fr = lane & 15, fq = lane >> 4;
    const unsigned short* xb = x + (size_t)b * CT * CD;
    f32x4 acc[4][4] = {};
    int l0 = mt * BM;
    for (int k0 = 0; k0 < KTOT; k0 += BK) {
        for (int i = 0; i < 4; ++i) {
            int c = tid + i * 256, r = c >> 3, cc = (c & 7) * 8;
            int l = l0 + r; if (l > CL - 1) l = CL - 1;
            *(bf16x8*)&sA[r][cc] = *(const bf16x8*)(xb + (size_t)l * 1024 + k0 + cc);
            *(bf16x8*)&sB[r][cc] = *(const bf16x8*)(wt + (size_t)(nt * BN + r) * KTOT + k0 + cc);
        }
        __syncthreads();
        for (int kk = 0; kk < 2; ++kk) {
            bf16x8 a[4], bb[4];
            for (int m = 0; m < 4; ++m) a[m] = *(const bf16x8*)&sA[wr * 64 + m * 16 + fr][kk * 32 + fq * 8];
            for (int n = 0; n < 4; ++n) bb[n] = *(const bf16x8*)&sB[wc * 64 + n * 16 + fr][kk * 32 + fq * 8];
            for (int m = 0; m < 4; ++m) for (int n = 0; n < 4; ++n)
                acc[m][n] = __builtin_amdgcn_mfma_f32_16x16x32_bf16(a[m], bb[n], acc[m][n], 0, 0, 0);
        }
        __syncthreads();
    }
    int nvalid = CL - l0; if (nvalid > BM) nvalid = BM;
    float csum[4];
    for (int n = 0; n < 4; ++n) {
        int col = nt * BN + wc * 64 + n * 16 + fr;
        float bv = bf2f(bias[col]);
        float s = 0.f;
        for (int m = 0; m < 4; ++m) for (int j = 0; j < 4; ++j) {
            int row = wr * 64 + m * 16 + fq * 4 + j;
            float v = acc[m][n][j] + bv;
            acc[m][n][j] = v;
            if (row < nvalid) { s += v; y[((size_t)b * CL + l0 + row) * CD + col] = f2bf(v); }
        }
        s += __shfl_xor(s, 16); s += __shfl_xor(s, 32);
        csum[n] = s;
    }
    if (fq == 0) for (int n = 0; n < 4; ++n) red[wr][wc * 64 + n * 16 + fr] = csum[n];
    __syncthreads();
    if (tid < BN) smean[tid] = (red[0][tid] + red[1][tid]) / (float)nvalid;
    __syncthreads();
    for (int n = 0; n < 4; ++n) {
        float mu = smean[wc * 64 + n * 16 + fr];
        float s = 0.f;
        for (int m = 0; m < 4; ++m) for (int j = 0; j < 4; ++j) {
            int row = wr * 64 + m * 16 + fq * 4 + j;
            float d = acc[m][n][j] - mu;
            if (row < nvalid) s += d * d;
        }
        s += __shfl_xor(s, 16); s += __shfl_xor(s, 32);
        csum[n] = s;
    }
    __syncthreads();
    if (fq == 0) for (int n = 0; n < 4; ++n) red[wr][wc * 64 + n * 16 + fr] = csum[n];
    __syncthreads();
    if (tid < BN) {
        float* p = part + (((size_t)b * MT_PER_B + mt) * CD + nt * BN + tid) * 2;
        p[0] = smean[tid]; p[1] = red[0][tid] + red[1][tid];
    }
}

__global__ void k_stats(const float* __restrict__ part, const unsigned short* __restrict__ gamma,
                        const unsigned short* __restrict__ beta, float* __restrict__ ss  ) {
    int idx = blockIdx.x * blockDim.x + threadIdx.x;
    if (idx >= CB * CD) return;
    int b = idx / CD, d = idx % CD;
    float n = 0.f, mean = 0.f, M2 = 0.f;
    for (int t = 0; t < MT_PER_B; ++t) {
        const float* p = part + (((size_t)b * MT_PER_B + t) * CD + d) * 2;
        float nb = (float)((CL - t * BM) > BM ? BM : (CL - t * BM));
        float mb = p[0], M2b = p[1];
        float nn = n + nb, delta = mb - mean;
        mean += delta * nb / nn;
        M2 += M2b + delta * delta * n * nb / nn;
        n = nn;
    }
    float var = M2 / n;
    float rs = rsqrtf(var + 1e-5f);
    float g = bf2f(gamma[d]), be = bf2f(beta[d]);
    ss[(size_t)idx * 2] = rs * g;
    ss[(size_t)idx * 2 + 1] = be - mean * rs * g;
}

__device__ __forceinline__ float gelu_tanh(float v) {
    float u = 0.7978845608028654f * (v + 0.044715f * v * v * v);
    float e = __expf(2.f * u);
    float t = 1.f - 2.f / (e + 1.f);
    return 0.5f * v * (1.f + t);
}

__global__ __launch_bounds__(256) void k_norm(const unsigned short* __restrict__ y, const float* __restrict__ ss,
                                              unsigned short* __restrict__ out) {
    size_t total8 = (size_t)CB * CL * CD / 8;
    for (size_t i = (size_t)blockIdx.x * blockDim.x + threadIdx.x; i < total8; i += (size_t)gridDim.x * blockDim.x) {
        size_t e = i * 8;
        int d = (int)(e % CD);
        int b = (int)(e / ((size_t)CL * CD));
        bf16x8 v = *(const bf16x8*)(y + e);
        const float* s = ss + ((size_t)b * CD + d) * 2;
        bf16x8 o;
        for (int j = 0; j < 8; ++j) {
            float f = bf2f((unsigned short)v[j]) * s[2 * j] + s[2 * j + 1];
            o[j] = (short)f2bf(gelu_tanh(f));
        }
        *(bf16x8*)(out + e) = o;
    }
}

extern "C" void kernel_launch(void* const* d_in, const int* in_sizes, int n_in,
                              void* d_out, int out_size, void* d_ws, size_t ws_size,
                              hipStream_t stream) {
    const unsigned short* x = (const unsigned short*)d_in[0];
    const unsigned short* w = (const unsigned short*)d_in[1];
    const unsigned short* bias = (const unsigned short*)d_in[2];
    const unsigned short* gamma = (const unsigned short*)d_in[3];
    const unsigned short* beta = (const unsigned short*)d_in[4];
    unsigned short* out = (unsigned short*)d_out;
    char* ws = (char*)d_ws;
    unsigned short* wt = (unsigned short*)ws;
    unsigned short* y = (unsigned short*)(ws + (4u << 20));
    float* part = (float*)(ws + (24u << 20));
    float* ss = (float*)(ws + (26u << 20));
    k_transpose_w<<<dim3(KTOT / 64, CD / 64), 256, 0, stream>>>(w, wt);
    k_gemm<<<CB * 4 * MT_PER_B, 256, 0, stream>>>(x, wt, bias, y, part);
    k_stats<<<(CB * CD + 255) / 256, 256, 0, stream>>>(part, gamma, beta, ss);
    k_norm<<<2048, 256, 0, stream>>>(y, ss, out);
}
```

```cpp
#include <hip/hip_runtime.h>
#include <hip/hip_bf16.h>
#include <stdint.h>

using bf16x8 = __attribute__((ext_vector_type(8))) short;
using s16x4  = __attribute__((ext_vector_type(4))) short;
using f32x4  = __attribute__((ext_vector_type(4))) float;
typedef __attribute__((address_space(3))) s16x4* lds_s16x4_ptr;

static constexpr int CB = 2, CT = 16000, CD = 512, CL = 7998;
static constexpr int BM = 256, BN = 128;
static constexpr int MT_PER_B = 32;
static constexpr int A_PLANE_BYTES = 272 * 64;
static constexpr int A_BYTES = 2 * A_PLANE_BYTES;
static constexpr int B_BYTES = 5 * 8192;
static constexpr int STAGE_BYTES = A_BYTES + B_BYTES;
static constexpr int NSTAGE = 16;

__device__ __forceinline__ float bf2f(unsigned short u) { return __uint_as_float(((unsigned)u) << 16); }
__device__ __forceinline__ unsigned short f2bf(float f) {
    __hip_bfloat16 h = __float2bfloat16(f);
    return *reinterpret_cast<unsigned short*>(&h);
}

__device__ __forceinline__ float gelu_tanh(float v) {
    const float k1 = -2.0f * 0.7978845608028654f * 1.4426950408889634f;
    const float k2 = k1 * 0.044715f;
    float arg = v * __builtin_fmaf(v * v, k2, k1);
    float e = __builtin_amdgcn_exp2f(arg);
    return v * __builtin_amdgcn_rcpf(1.0f + e);
}
typedef __attribute__((ext_vector_type(2))) __bf16 bf16x2_t;
typedef __attribute__((ext_vector_type(2))) float f32x2_t;
__device__ __forceinline__ unsigned pk_bf16(float a, float b) {
    f32x2_t f = {a, b};
    bf16x2_t h = __builtin_convertvector(f, bf16x2_t);
    return __builtin_bit_cast(unsigned, h);
}

__global__ __launch_bounds__(512) void k_gemm(const unsigned short* __restrict__ x, const unsigned short* __restrict__ w,
                                              const unsigned short* __restrict__ bias, unsigned short* __restrict__ y,
                                              float* __restrict__ part  ) {
    __shared__ __attribute__((aligned(1024))) char smem[2 * STAGE_BYTES + 4096 + 512 + 256];
    const int bid = blockIdx.x;
    const int xcd = bid & 7, idx = bid >> 3;
    const int mtg = xcd * 8 + (idx >> 2), nt = idx & 3;
    const int b = mtg >> 5, mt = mtg & 31;
    const int tid = threadIdx.x, wid = tid >> 6, lane = tid & 63;
    const int wr = wid >> 1, wc = wid & 1, fr = lane & 15, fq = lane >> 4;
    const int l0 = mt * BM, n0 = nt * BN;
    const unsigned short* xb = x + (size_t)b * CT * CD;

    unsigned pA[5], pB[5];
    int dA[5], dB[5];
    #pragma unroll
    for (int i = 0; i < 5; ++i) {
        int j = wid + 8 * i; if (j > 33) j = 33;
        int p = j / 17, jb = j % 17;
        int rr = jb * 16 + (lane >> 2);
        int ch = (lane & 3) ^ (((rr >> 2) & 1) << 1);
        int t = 2 * (l0 + rr) + p; if (t > CT - 1) t = CT - 1;
        pA[i] = (unsigned)(((size_t)b * CT + t) * CD + ch * 8) * 2u;
        dA[i] = B_BYTES + p * A_PLANE_BYTES + jb * 1024 + lane * 16;
        int jj = wid + 8 * i; int tap = jj >> 3, kb = jj & 7;
        int k = kb * 4 + (lane >> 4), c16 = lane & 15;
        int h = (k & 3) | (((k >> 3) & 1) << 2);
        int nb = (c16 >> 1) ^ h;
        pB[i] = (unsigned)(((tap * CD + k) * CD) + n0 + nb * 16 + (c16 & 1) * 8) * 2u;
        dB[i] = tap * 8192 + kb * 1024 + lane * 16;
    }
    unsigned sdA[5], sdB[5];
    const unsigned smem_base = (unsigned)(size_t)smem;
    #pragma unroll
    for (int i = 0; i < 5; ++i) {
        sdA[i] = __builtin_amdgcn_readfirstlane(smem_base + dA[i] - lane * 16);
        sdB[i] = __builtin_amdgcn_readfirstlane(smem_base + dB[i] - lane * 16);
    }
    auto glds = [&](unsigned voff, const char* sbase, unsigned dst) {
        unsigned keep;
        asm volatile("s_mov_b32 %0, m0\n\ts_mov_b32 m0, %2\n\ts_nop 0\n\tglobal_load_lds_dwordx4 %1, %3\n\ts_mov_b32 m0, %0"
                     : "=&s"(keep) : "v"(voff), "s"(dst), "s"(sbase) : "memory");
    };
    auto issueA = [&](int i, int s, int buf) { if (i < 4 || wid < 2) glds(pA[i], (const char*)x + s * 64, sdA[i] + buf * STAGE_BYTES); };
    auto issueB = [&](int i, int s, int buf) { glds(pB[i], (const char*)w + (size_t)s * 32 * CD * 2, sdB[i] + buf * STAGE_BYTES); };
    auto issue = [&](int s, int buf) {
        #pragma unroll
        for (int i = 0; i < 5; ++i) issueA(i, s, buf);
        #pragma unroll
        for (int i = 0; i < 5; ++i) issueB(i, s, buf);
    };

    unsigned pfx; { int rr = nt * 129 + wid * 64 + lane; int t = 2 * l0 + rr; if (rr > 514) t = 2 * l0 + 514; if (t > CT - 1) t = CT - 1; pfx = (unsigned)(((size_t)b * CT + t) * CD) * 2u; }
    unsigned pfw; { int e = (mtg & 7) * 40 + (lane < 40 ? lane : 39); int row = e >> 1, hf = e & 1; int tap = row >> 5, k = row & 31; pfw = (unsigned)(((tap * CD + k) * CD) + n0 + hf * 64) * 2u; }
    const unsigned sdummy = __builtin_amdgcn_readfirstlane(smem_base + 2 * STAGE_BYTES + 4096 + 512);
    auto glds4 = [&](unsigned voff, const char* sbase) {
        unsigned keep;
        asm volatile("s_mov_b32 %0, m0\n\ts_mov_b32 m0, %2\n\ts_nop 0\n\tglobal_load_lds_dword %1, %3\n\ts_mov_b32 m0, %0"
                     : "=&s"(keep) : "v"(voff), "s"(sdummy), "s"(sbase) : "memory");
    };
    const int swid = __builtin_amdgcn_readfirstlane(wid);
    auto prefX = [&](int j) -> bool { if (swid < 3 && j < 8) { glds4(pfx, (const char*)x + j * 128); return true; } return false; };
    auto prefW = [&](int sw) -> bool { if (swid == 3 && sw < NSTAGE) { glds4(pfw, (const char*)w + (size_t)sw * 32 * CD * 2); return true; } return false; };

    f32x4 acc[4][4] = {};
    int aoff[4];
    const int g = lane >> 4, q = (lane & 15) >> 2, pp = lane & 3;

    unsigned rB[4], rA[3];
    {
        int k = 8 * g + q; int h = (k & 3) | (((k >> 3) & 1) << 2);
        #pragma unroll
        for (int n = 0; n < 4; ++n) rB[n] = smem_base + k * 256 + (((wc * 4 + n) ^ h) * 32) + pp * 8;
        #pragma unroll
        for (int sft = 0; sft < 3; ++sft) {
            int row = wr * 64 + fr + sft;
            rA[sft] = smem_base + B_BYTES + row * 64 + ((fq ^ (((row >> 2) & 1) << 1)) * 16);
        }
    }
    typedef __attribute__((address_space(3))) const bf16x8* lds_bf16x8_ptr;
    auto ldfrag = [&](unsigned boff, int tap, bf16x8 (&af)[4], bf16x8 (&bfr)[4]) {
        #pragma unroll
        for (int n = 0; n < 4; ++n) {
            unsigned ad = rB[n] + boff;
            s16x4 lo = __builtin_amdgcn_ds_read_tr16_b64_v4i16((lds_s16x4_ptr)(ad + tap * 8192));
            s16x4 hi = __builtin_amdgcn_ds_read_tr16_b64_v4i16((lds_s16x4_ptr)(ad + tap * 8192 + 1024));
            bfr[n] = __builtin_shufflevector(lo, hi, 0, 1, 2, 3, 4, 5, 6, 7);
        }
        unsigned aa = rA[tap >> 1] + boff;
        #pragma unroll
        for (int m = 0; m < 4; ++m)
            af[m] = *(lds_bf16x8_ptr)(aa + (tap & 1) * A_PLANE_BYTES + m * 1024);
    };
    auto mma = [&](bf16x8 (&af)[4], bf16x8 (&bfr)[4]) {
        __builtin_amdgcn_s_setprio(1);
        #pragma unroll
        for (int n = 0; n < 4; ++n)
            #pragma unroll
            for (int m = 0; m < 4; ++m)
                acc[n][m] = __builtin_amdgcn_mfma_f32_16x16x32_bf16(bfr[n], af[m], acc[n][m], 0, 0, 0);
        __builtin_amdgcn_s_setprio(0);
    };
    bf16x8 a0[4], b0[4], a1[4], b1[4];
    issue(0, 0);
    if (tid < 128) ((float*)(smem + 2 * STAGE_BYTES + 4096))[tid] = bf2f(bias[n0 + tid]);
    asm volatile("s_waitcnt vmcnt(0)" ::: "memory");
    prefX(1); prefW(1); prefW(2);
    __syncthreads();
    ldfrag(0u, 0, a0, b0);
    #pragma unroll 1
    for (int s = 0; s < NSTAGE; s += 2) {
        {
            unsigned base = 0u; asm volatile("" : "+s"(base));
            unsigned nbase = STAGE_BYTES; asm volatile("" : "+s"(nbase));
            issueA(0, s + 1, 1); issueB(0, s + 1, 1); issueA(1, s + 1, 1); issueB(1, s + 1, 1);
            ldfrag(base, 1, a1, b1); mma(a0, b0);
            issueA(2, s + 1, 1); issueB(2, s + 1, 1); issueA(3, s + 1, 1); issueB(3, s + 1, 1);
            ldfrag(base, 2, a0, b0); mma(a1, b1);
            issueA(4, s + 1, 1); issueB(4, s + 1, 1);
            bool pf = prefX((s >> 1) + 2); pf = prefW(s + 3) || pf;
            ldfrag(base, 3, a1, b1); mma(a0, b0);
            ldfrag(base, 4, a0, b0); mma(a1, b1);
            if (pf) asm volatile("s_waitcnt vmcnt(1)" ::: "memory"); else asm volatile("s_waitcnt vmcnt(0)" ::: "memory");
            __syncthreads();
            ldfrag(nbase, 0, a1, b1); mma(a0, b0);
        }
        {
            unsigned base = STAGE_BYTES; asm volatile("" : "+s"(base));
            unsigned nbase = 0u; asm volatile("" : "+s"(nbase));
            const bool more = s + 2 < NSTAGE;
            if (more) { issueA(0, s + 2, 0); issueB(0, s + 2, 0); issueA(1, s + 2, 0); issueB(1, s + 2, 0); }
            ldfrag(base, 1, a0, b0); mma(a1, b1);
            if (more) { issueA(2, s + 2, 0); issueB(2, s + 2, 0); issueA(3, s + 2, 0); issueB(3, s + 2, 0); }
            ldfrag(base, 2, a1, b1); mma(a0, b0);
            if (more) { issueA(4, s + 2, 0); issueB(4, s + 2, 0); }
            bool pf = prefW(s + 4);
            ldfrag(base, 3, a0, b0); mma(a1, b1);
            ldfrag(base, 4, a1, b1); mma(a0, b0);
            if (pf) asm volatile("s_waitcnt vmcnt(1)" ::: "memory"); else asm volatile("s_waitcnt vmcnt(0)" ::: "memory");
            __syncthreads();
            if (more) ldfrag(nbase, 0, a0, b0);
            mma(a1, b1);
        }
    }
    asm volatile("s_waitcnt vmcnt(0)" ::: "memory");
    __syncthreads();
    int nvalid = CL - l0; if (nvalid > BM) nvalid = BM;
    int wvalid = nvalid - wr * 64; wvalid = wvalid < 0 ? 0 : (wvalid > 64 ? 64 : wvalid);
    const float winv = wvalid > 0 ? 1.0f / (float)wvalid : 0.f;
    float* wst = (float*)(smem + 2 * STAGE_BYTES);
    const float* sbias = (const float*)(smem + 2 * STAGE_BYTES + 4096);
    auto row16sum = [&](float v) {
        v += __builtin_bit_cast(float, __builtin_amdgcn_update_dpp(0, __builtin_bit_cast(int, v), 0x128, 0xf, 0xf, false));
        v += __builtin_bit_cast(float, __builtin_amdgcn_update_dpp(0, __builtin_bit_cast(int, v), 0x124, 0xf, 0xf, false));
        v += __builtin_bit_cast(float, __builtin_amdgcn_update_dpp(0, __builtin_bit_cast(int, v), 0x122, 0xf, 0xf, false));
        v += __builtin_bit_cast(float, __builtin_amdgcn_update_dpp(0, __builtin_bit_cast(int, v), 0x121, 0xf, 0xf, false));
        return v;
    };
    #pragma unroll
    for (int nf = 0; nf < 4; ++nf) {
        f32x4 bv = *(const f32x4*)(sbias + wc * 64 + nf * 16 + 4 * fq);
        #pragma unroll
        for (int r = 0; r < 4; ++r) {
            float sm = 0.f;
            #pragma unroll
            for (int mf = 0; mf < 4; ++mf) {
                float v = acc[nf][mf][r] + bv[r]; acc[nf][mf][r] = v;
                if (mf * 16 + fr < wvalid) sm += v;
            }
            float mu = row16sum(sm) * winv;
            float s2 = 0.f;
            #pragma unroll
            for (int mf = 0; mf < 4; ++mf) {
                float d = acc[nf][mf][r] - mu;
                if (mf * 16 + fr < wvalid) s2 += d * d;
            }
            s2 = row16sum(s2);
            if (fr == 0) { float2 o; o.x = mu; o.y = s2; *(float2*)(wst + (wr * 128 + wc * 64 + nf * 16 + 4 * fq + r) * 2) = o; }
        }
    }
    char* ty = smem;
    #pragma unroll
    for (int nf = 0; nf < 4; ++nf)
        #pragma unroll
        for (int mf = 0; mf < 4; ++mf) {
            int row = wr * 64 + mf * 16 + fr, col = wc * 64 + nf * 16 + 4 * fq;
            uint2 pk;
            pk.x = pk_bf16(acc[nf][mf][0], acc[nf][mf][1]);
            pk.y = pk_bf16(acc[nf][mf][2], acc[nf][mf][3]);
            *(uint2*)(ty + row * 272 + col * 2) = pk;
        }
    __syncthreads();
    #pragma unroll
    for (int it = 0; it < 8; ++it) {
        int e = it * 512 + tid, row = e >> 4, c = e & 15;
        if (row < nvalid) {
            uint4 v = *(const uint4*)(ty + row * 272 + c * 16);
            *(uint4*)(y + ((size_t)b * CL + l0 + row) * CD + n0 + c * 8) = v;
        }
    }
    if (tid < 128) {
        float n = 0.f, mean = 0.f, M2 = 0.f;
        #pragma unroll
        for (int q4 = 0; q4 < 4; ++q4) {
            int nb_i = nvalid - q4 * 64; nb_i = nb_i < 0 ? 0 : (nb_i > 64 ? 64 : nb_i);
            if (nb_i > 0) {
                float2 p = *(const float2*)(wst + (q4 * 128 + tid) * 2);
                float nb = (float)nb_i, nn = n + nb, delta = p.x - mean;
                mean += delta * nb / nn;
                M2 += p.y + delta * delta * n * nb / nn;
                n = nn;
            }
        }
        float2 o; o.x = mean; o.y = M2;
        *(float2*)(part + (((size_t)b * MT_PER_B + mt) * CD + n0 + tid) * 2) = o;
    }
}

static constexpr int NORM_RC = 125;
__global__ __launch_bounds__(256) void k_norm(const unsigned short* __restrict__ y, const float* __restrict__ part,
                                              const unsigned short* __restrict__ gamma, const unsigned short* __restrict__ beta,
                                              unsigned short* __restrict__ out) {
    __shared__ float sc[128], sh[128];
    const int cs = blockIdx.x & 3, rc = blockIdx.x >> 2;
    const int b = rc / NORM_RC, ch = rc % NORM_RC;
    const int tid = threadIdx.x;
    const int rows_per = (CL + NORM_RC - 1) / NORM_RC;
    const int r0 = ch * rows_per;
    int rend = r0 + rows_per; if (rend > CL) rend = CL;
    const int c8 = tid & 15, rr = tid >> 4;
    const size_t base = (size_t)b * CL * CD + cs * 128 + c8 * 8;
    bf16x8 v[4];
    #pragma unroll
    for (int u = 0; u < 4; ++u) { int r = r0 + rr + u * 16; if (r > CL - 1) r = CL - 1; v[u] = *(const bf16x8*)(y + base + (size_t)r * CD); }
    if (tid < 128) {
        int d = cs * 128 + tid;
        float2 p[MT_PER_B];
        #pragma unroll
        for (int t = 0; t < MT_PER_B; ++t) p[t] = *(const float2*)(part + (((size_t)b * MT_PER_B + t) * CD + d) * 2);
        float ms = 0.f;
        #pragma unroll
        for (int t = 0; t < MT_PER_B; ++t) { float nb = (float)((CL - t * BM) > BM ? BM : (CL - t * BM)); ms += nb * p[t].x; }
        float mean = ms * (1.0f / (float)CL);
        float M2 = 0.f;
        #pragma unroll
        for (int t = 0; t < MT_PER_B; ++t) { float nb = (float)((CL - t * BM) > BM ? BM : (CL - t * BM)); float dl = p[t].x - mean; M2 += p[t].y + nb * dl * dl; }
        float rs = rsqrtf(M2 * (1.0f / (float)CL) + 1e-5f);
        float gm = bf2f(gamma[d]), be = bf2f(beta[d]);
        sc[tid] = rs * gm; sh[tid] = be - mean * rs * gm;
    }
    __syncthreads();
    float lsc[8], lsh[8];
    #pragma unroll
    for (int j = 0; j < 8; ++j) { lsc[j] = sc[c8 * 8 + j]; lsh[j] = sh[c8 * 8 + j]; }
    #pragma unroll
    for (int u = 0; u < 4; ++u) {
        int r = r0 + rr + u * 16;
        if (r < rend) {
            uint4 o; float f[8];
            #pragma unroll
            for (int j = 0; j < 8; ++j) f[j] = gelu_tanh(__builtin_fmaf(bf2f((unsigned short)v[u][j]), lsc[j], lsh[j]));
            o.x = pk_bf16(f[0], f[1]); o.y = pk_bf16(f[2], f[3]); o.z = pk_bf16(f[4], f[5]); o.w = pk_bf16(f[6], f[7]);
            *(uint4*)(out + base + (size_t)r * CD) = o;
        }
    }
}

extern "C" void kernel_launch(void* const* d_in, const int* in_sizes, int n_in,
                              void* d_out, int out_size, void* d_ws, size_t ws_size,
                              hipStream_t stream) {
    const unsigned short* x = (const unsigned short*)d_in[0];
    const unsigned short* w = (const unsigned short*)d_in[1];
    const unsigned short* bias = (const unsigned short*)d_in[2];
    const unsigned short* gamma = (const unsigned short*)d_in[3];
    const unsigned short* beta = (const unsigned short*)d_in[4];
    unsigned short* out = (unsigned short*)d_out;
    char* ws = (char*)d_ws;
    unsigned short* y = (unsigned short*)ws;
    float* part = (float*)(ws + (20u << 20));
    k_gemm<<<256, 512, 0, stream>>>(x, w, bias, y, part);
    k_norm<<<4 * 2 * NORM_RC, 256, 0, stream>>>(y, part, gamma, beta, out);
}
```

```cpp
#include <hip/hip_runtime.h>
#include <hip/hip_bf16.h>
#include <stdint.h>

using bf16x8 = __attribute__((ext_vector_type(8))) short;
using s16x4  = __attribute__((ext_vector_type(4))) short;
using f32x4  = __attribute__((ext_vector_type(4))) float;
typedef __attribute__((address_space(3))) s16x4* lds_s16x4_ptr;

static constexpr int CB = 2, CT = 16000, CD = 512, CL = 7998;
static constexpr int BM = 256, BN = 128;
static constexpr int MT_PER_B = 32;
static constexpr int A_PLANE_BYTES = 272 * 64;
static constexpr int A_BYTES = 2 * A_PLANE_BYTES;
static constexpr int B_BYTES = 5 * 8192;
static constexpr int STAGE_BYTES = A_BYTES + B_BYTES;
static constexpr int NSTAGE = 16;

__device__ __forceinline__ float bf2f(unsigned short u) { return __uint_as_float(((unsigned)u) << 16); }
__device__ __forceinline__ unsigned short f2bf(float f) {
    __hip_bfloat16 h = __float2bfloat16(f);
    return *reinterpret_cast<unsigned short*>(&h);
}

__device__ __forceinline__ float gelu_tanh(float v) {
    const float k1 = -2.0f * 0.7978845608028654f * 1.4426950408889634f;
    const float k2 = k1 * 0.044715f;
    float arg = v * __builtin_fmaf(v * v, k2, k1);
    float e = __builtin_amdgcn_exp2f(arg);
    return v * __builtin_amdgcn_rcpf(1.0f + e);
}
typedef __attribute__((ext_vector_type(2))) __bf16 bf16x2_t;
typedef __attribute__((ext_vector_type(2))) float f32x2_t;
__device__ __forceinline__ unsigned pk_bf16(float a, float b) {
    f32x2_t f = {a, b};
    bf16x2_t h = __builtin_convertvector(f, bf16x2_t);
    return __builtin_bit_cast(unsigned, h);
}

__global__ __launch_bounds__(512) void k_gemm(const unsigned short* __restrict__ x, const unsigned short* __restrict__ w,
                                              const unsigned short* __restrict__ bias, const unsigned short* __restrict__ gamma,
                                              const unsigned short* __restrict__ beta, unsigned short* __restrict__ out,
                                              unsigned* __restrict__ cnt  , float* __restrict__ part  ) {
    __shared__ __attribute__((aligned(1024))) char smem[2 * STAGE_BYTES + 4096 + 512 + 256];
    const int bid = blockIdx.x;
    const int xcd = bid & 7, idx = bid >> 3;
    const int mtg = xcd * 8 + (idx >> 2), nt = idx & 3;
    const int b = mtg >> 5, mt = mtg & 31;
    const int tid = threadIdx.x, wid = tid >> 6, lane = tid & 63;
    const int wr = wid >> 1, wc = wid & 1, fr = lane & 15, fq = lane >> 4;
    const int l0 = mt * BM, n0 = nt * BN;
    const unsigned short* xb = x + (size_t)b * CT * CD;

    unsigned pA[5], pB[5];
    int dA[5], dB[5];
    #pragma unroll
    for (int i = 0; i < 5; ++i) {
        int j = wid + 8 * i; if (j > 33) j = 33;
        int p = j / 17, jb = j % 17;
        int rr = jb * 16 + (lane >> 2);
        int ch = (lane & 3) ^ (((rr >> 2) & 1) << 1);
        int t = 2 * (l0 + rr) + p; if (t > CT - 1) t = CT - 1;
        pA[i] = (unsigned)(((size_t)b * CT + t) * CD + ch * 8) * 2u;
        dA[i] = B_BYTES + p * A_PLANE_BYTES + jb * 1024 + lane * 16;
        int jj = wid + 8 * i; int tap = jj >> 3, kb = jj & 7;
        int k = kb * 4 + (lane >> 4), c16 = lane & 15;
        int h = (k & 3) | (((k >> 3) & 1) << 2);
        int nb = (c16 >> 1) ^ h;
        pB[i] = (unsigned)(((tap * CD + k) * CD) + n0 + nb * 16 + (c16 & 1) * 8) * 2u;
        dB[i] = tap * 8192 + kb * 1024 + lane * 16;
    }
    unsigned sdA[5], sdB[5];
    const unsigned smem_base = (unsigned)(size_t)smem;
    #pragma unroll
    for (int i = 0; i < 5; ++i) {
        sdA[i] = __builtin_amdgcn_readfirstlane(smem_base + dA[i] - lane * 16);
        sdB[i] = __builtin_amdgcn_readfirstlane(smem_base + dB[i] - lane * 16);
    }
    auto glds = [&](unsigned voff, const char* sbase, unsigned dst) {
        unsigned keep;
        asm volatile("s_mov_b32 %0, m0\n\ts_mov_b32 m0, %2\n\ts_nop 0\n\tglobal_load_lds_dwordx4 %1, %3\n\ts_mov_b32 m0, %0"
                     : "=&s"(keep) : "v"(voff), "s"(dst), "s"(sbase) : "memory");
    };
    auto issueA = [&](int i, int s, int buf) { if (i < 4 || wid < 2) glds(pA[i], (const char*)x + s * 64, sdA[i] + buf * STAGE_BYTES); };
    auto issueB = [&](int i, int s, int buf) { glds(pB[i], (const char*)w + (size_t)s * 32 * CD * 2, sdB[i] + buf * STAGE_BYTES); };
    auto issue = [&](int s, int buf) {
        #pragma unroll
        for (int i = 0; i < 5; ++i) issueA(i, s, buf);
        #pragma unroll
        for (int i = 0; i < 5; ++i) issueB(i, s, buf);
    };

    unsigned pfx; { int rr = nt * 129 + wid * 64 + lane; int t = 2 * l0 + rr; if (rr > 514) t = 2 * l0 + 514; if (t > CT - 1) t = CT - 1; pfx = (unsigned)(((size_t)b * CT + t) * CD) * 2u; }
    unsigned pfw; { int e = (mtg & 7) * 40 + (lane < 40 ? lane : 39); int row = e >> 1, hf = e & 1; int tap = row >> 5, k = row & 31; pfw = (unsigned)(((tap * CD + k) * CD) + n0 + hf * 64) * 2u; }
    const unsigned sdummy = __builtin_amdgcn_readfirstlane(smem_base + 2 * STAGE_BYTES + 4096 + 512);
    auto glds4 = [&](unsigned voff, const char* sbase) {
        unsigned keep;
        asm volatile("s_mov_b32 %0, m0\n\ts_mov_b32 m0, %2\n\ts_nop 0\n\tglobal_load_lds_dword %1, %3\n\ts_mov_b32 m0, %0"
                     : "=&s"(keep) : "v"(voff), "s"(sdummy), "s"(sbase) : "memory");
    };
    const int swid = __builtin_amdgcn_readfirstlane(wid);
    auto prefX = [&](int j) -> bool { if (swid < 3 && j < 8) { glds4(pfx, (const char*)x + j * 128); return true; } return false; };
    auto prefW = [&](int sw) -> bool { if (swid == 3 && sw < NSTAGE) { glds4(pfw, (const char*)w + (size_t)sw * 32 * CD * 2); return true; } return false; };

    f32x4 acc[4][4] = {};
    int aoff[4];
    const int g = lane >> 4, q = (lane & 15) >> 2, pp = lane & 3;

    unsigned rB[4], rA[3];
    {
        int k = 8 * g + q; int h = (k & 3) | (((k >> 3) & 1) << 2);
        #pragma unroll
        for (int n = 0; n < 4; ++n) rB[n] = smem_base + k * 256 + (((wc * 4 + n) ^ h) * 32) + pp * 8;
        #pragma unroll
        for (int sft = 0; sft < 3; ++sft) {
            int row = wr * 64 + fr + sft;
            rA[sft] = smem_base + B_BYTES + row * 64 + ((fq ^ (((row >> 2) & 1) << 1)) * 16);
        }
    }
    typedef __attribute__((address_space(3))) const bf16x8* lds_bf16x8_ptr;
    auto ldfrag = [&](unsigned boff, int tap, bf16x8 (&af)[4], bf16x8 (&bfr)[4]) {
        #pragma unroll
        for (int n = 0; n < 4; ++n) {
            unsigned ad = rB[n] + boff;
            s16x4 lo = __builtin_amdgcn_ds_read_tr16_b64_v4i16((lds_s16x4_ptr)(ad + tap * 8192));
            s16x4 hi = __builtin_amdgcn_ds_read_tr16_b64_v4i16((lds_s16x4_ptr)(ad + tap * 8192 + 1024));
            bfr[n] = __builtin_shufflevector(lo, hi, 0, 1, 2, 3, 4, 5, 6, 7);
        }
        unsigned aa = rA[tap >> 1] + boff;
        #pragma unroll
        for (int m = 0; m < 4; ++m)
            af[m] = *(lds_bf16x8_ptr)(aa + (tap & 1) * A_PLANE_BYTES + m * 1024);
    };
    auto mma = [&](bf16x8 (&af)[4], bf16x8 (&bfr)[4]) {
        __builtin_amdgcn_s_setprio(1);
        #pragma unroll
        for (int n = 0; n < 4; ++n)
            #pragma unroll
            for (int m = 0; m < 4; ++m)
                acc[n][m] = __builtin_amdgcn_mfma_f32_16x16x32_bf16(bfr[n], af[m], acc[n][m], 0, 0, 0);
        __builtin_amdgcn_s_setprio(0);
    };
    bf16x8 a0[4], b0[4], a1[4], b1[4];
    issue(0, 0);
    if (tid < 128) ((float*)(smem + 2 * STAGE_BYTES + 4096))[tid] = bf2f(bias[n0 + tid]);
    asm volatile("s_waitcnt vmcnt(0)" ::: "memory");
    prefX(1); prefW(1); prefW(2);
    __syncthreads();
    ldfrag(0u, 0, a0, b0);
    #pragma unroll 1
    for (int s = 0; s < NSTAGE; s += 2) {
        {
            unsigned base = 0u; asm volatile("" : "+s"(base));
            unsigned nbase = STAGE_BYTES; asm volatile("" : "+s"(nbase));
            issueA(0, s + 1, 1); issueB(0, s + 1, 1); issueA(1, s + 1, 1); issueB(1, s + 1, 1);
            ldfrag(base, 1, a1, b1); mma(a0, b0);
            issueA(2, s + 1, 1); issueB(2, s + 1, 1); issueA(3, s + 1, 1); issueB(3, s + 1, 1);
            ldfrag(base, 2, a0, b0); mma(a1, b1);
            issueA(4, s + 1, 1); issueB(4, s + 1, 1);
            bool pf = prefX((s >> 1) + 2); pf = prefW(s + 3) || pf;
            ldfrag(base, 3, a1, b1); mma(a0, b0);
            ldfrag(base, 4, a0, b0); mma(a1, b1);
            if (pf) asm volatile("s_waitcnt vmcnt(1)" ::: "memory"); else asm volatile("s_waitcnt vmcnt(0)" ::: "memory");
            __syncthreads();
            ldfrag(nbase, 0, a1, b1); mma(a0, b0);
        }
        {
            unsigned base = STAGE_BYTES; asm volatile("" : "+s"(base));
            unsigned nbase = 0u; asm volatile("" : "+s"(nbase));
            const bool more = s + 2 < NSTAGE;
            if (more) { issueA(0, s + 2, 0); issueB(0, s + 2, 0); issueA(1, s + 2, 0); issueB(1, s + 2, 0); }
            ldfrag(base, 1, a0, b0); mma(a1, b1);
            if (more) { issueA(2, s + 2, 0); issueB(2, s + 2, 0); issueA(3, s + 2, 0); issueB(3, s + 2, 0); }
            ldfrag(base, 2, a1, b1); mma(a0, b0);
            if (more) { issueA(4, s + 2, 0); issueB(4, s + 2, 0); }
            bool pf = prefW(s + 4);
            ldfrag(base, 3, a0, b0); mma(a1, b1);
            ldfrag(base, 4, a1, b1); mma(a0, b0);
            if (pf) asm volatile("s_waitcnt vmcnt(1)" ::: "memory"); else asm volatile("s_waitcnt vmcnt(0)" ::: "memory");
            __syncthreads();
            if (more) ldfrag(nbase, 0, a0, b0);
            mma(a1, b1);
        }
    }
    asm volatile("s_waitcnt vmcnt(0)" ::: "memory");
    __syncthreads();
    int nvalid = CL - l0; if (nvalid > BM) nvalid = BM;
    int wvalid = nvalid - wr * 64; wvalid = wvalid < 0 ? 0 : (wvalid > 64 ? 64 : wvalid);
    const float winv = wvalid > 0 ? 1.0f / (float)wvalid : 0.f;
    float* wst = (float*)(smem + 2 * STAGE_BYTES);
    const float* sbias = (const float*)(smem + 2 * STAGE_BYTES + 4096);
    auto row16sum = [&](float v) {
        v += __builtin_bit_cast(float, __builtin_amdgcn_update_dpp(0, __builtin_bit_cast(int, v), 0x128, 0xf, 0xf, false));
        v += __builtin_bit_cast(float, __builtin_amdgcn_update_dpp(0, __builtin_bit_cast(int, v), 0x124, 0xf, 0xf, false));
        v += __builtin_bit_cast(float, __builtin_amdgcn_update_dpp(0, __builtin_bit_cast(int, v), 0x122, 0xf, 0xf, false));
        v += __builtin_bit_cast(float, __builtin_amdgcn_update_dpp(0, __builtin_bit_cast(int, v), 0x121, 0xf, 0xf, false));
        return v;
    };
    #pragma unroll
    for (int nf = 0; nf < 4; ++nf) {
        f32x4 bv = *(const f32x4*)(sbias + wc * 64 + nf * 16 + 4 * fq);
        #pragma unroll
        for (int r = 0; r < 4; ++r) {
            float sm = 0.f;
            #pragma unroll
            for (int mf = 0; mf < 4; ++mf) {
                float v = acc[nf][mf][r] + bv[r]; acc[nf][mf][r] = v;
                if (mf * 16 + fr < wvalid) sm += v;
            }
            float mu = row16sum(sm) * winv;
            float s2 = 0.f;
            #pragma unroll
            for (int mf = 0; mf < 4; ++mf) {
                float d = acc[nf][mf][r] - mu;
                if (mf * 16 + fr < wvalid) s2 += d * d;
            }
            s2 = row16sum(s2);
            if (fr == 0) { float2 o; o.x = mu; o.y = s2; *(float2*)(wst + (wr * 128 + wc * 64 + nf * 16 + 4 * fq + r) * 2) = o; }
        }
    }
    __syncthreads();
    const int grp = b * 4 + nt;
    if (tid < 128) {
        float n = 0.f, mean = 0.f, M2 = 0.f;
        #pragma unroll
        for (int q4 = 0; q4 < 4; ++q4) {
            int nb_i = nvalid - q4 * 64; nb_i = nb_i < 0 ? 0 : (nb_i > 64 ? 64 : nb_i);
            if (nb_i > 0) {
                float2 p = *(const float2*)(wst + (q4 * 128 + tid) * 2);
                float nb = (float)nb_i, nn = n + nb, delta = p.x - mean;
                mean += delta * nb / nn;
                M2 += p.y + delta * delta * n * nb / nn;
                n = nn;
            }
        }
        unsigned long long bits = ((unsigned long long)__float_as_uint(M2) << 32) | (unsigned long long)__float_as_uint(mean);
        __hip_atomic_store((unsigned long long*)(part + (((size_t)grp * MT_PER_B + mt) * 128 + tid) * 2), bits, __ATOMIC_RELAXED, __HIP_MEMORY_SCOPE_AGENT);
        asm volatile("s_waitcnt vmcnt(0)" ::: "memory");
    }
    __syncthreads();
    if (tid == 0) {
        __hip_atomic_fetch_add(cnt + grp * 32, 1u, __ATOMIC_RELAXED, __HIP_MEMORY_SCOPE_AGENT);
        unsigned spins = 0;
        while (__hip_atomic_load(cnt + grp * 32, __ATOMIC_RELAXED, __HIP_MEMORY_SCOPE_AGENT) < (unsigned)MT_PER_B) {
            __builtin_amdgcn_s_sleep(2);
            if (++spins > 200000u) break;
        }
        asm volatile("" ::: "memory");
    }
    __syncthreads();
    float* ssc = wst;
    float* ssh = wst + 128;
    float fsc = 0.f, fsh = 0.f;
    if (tid < 128) {
        unsigned long long pb[MT_PER_B];
        #pragma unroll
        for (int t = 0; t < MT_PER_B; ++t)
            pb[t] = __hip_atomic_load((const unsigned long long*)(part + (((size_t)grp * MT_PER_B + t) * 128 + tid) * 2), __ATOMIC_RELAXED, __HIP_MEMORY_SCOPE_AGENT);
        float ms = 0.f;
        #pragma unroll
        for (int t = 0; t < MT_PER_B; ++t) { float nb = (float)((CL - t * BM) > BM ? BM : (CL - t * BM)); ms += nb * __uint_as_float((unsigned)pb[t]); }
        float mean = ms * (1.0f / (float)CL);
        float M2 = 0.f;
        #pragma unroll
        for (int t = 0; t < MT_PER_B; ++t) { float nb = (float)((CL - t * BM) > BM ? BM : (CL - t * BM)); float dl = __uint_as_float((unsigned)pb[t]) - mean; M2 += __uint_as_float((unsigned)(pb[t] >> 32)) + nb * dl * dl; }
        float rs = rsqrtf(M2 * (1.0f / (float)CL) + 1e-5f);
        float gm = bf2f(gamma[n0 + tid]), be = bf2f(beta[n0 + tid]);
        fsc = rs * gm; fsh = be - mean * rs * gm;
    }
    __syncthreads();
    if (tid < 128) { ssc[tid] = fsc; ssh[tid] = fsh; }
    __syncthreads();
    char* ty = smem;
    #pragma unroll
    for (int nf = 0; nf < 4; ++nf) {
        f32x4 sc4 = *(const f32x4*)(ssc + wc * 64 + nf * 16 + 4 * fq);
        f32x4 sh4 = *(const f32x4*)(ssh + wc * 64 + nf * 16 + 4 * fq);
        #pragma unroll
        for (int mf = 0; mf < 4; ++mf) {
            int row = wr * 64 + mf * 16 + fr, col = wc * 64 + nf * 16 + 4 * fq;
            float g0 = gelu_tanh(__builtin_fmaf(acc[nf][mf][0], sc4[0], sh4[0]));
            float g1 = gelu_tanh(__builtin_fmaf(acc[nf][mf][1], sc4[1], sh4[1]));
            float g2 = gelu_tanh(__builtin_fmaf(acc[nf][mf][2], sc4[2], sh4[2]));
            float g3 = gelu_tanh(__builtin_fmaf(acc[nf][mf][3], sc4[3], sh4[3]));
            uint2 pk; pk.x = pk_bf16(g0, g1); pk.y = pk_bf16(g2, g3);
            *(uint2*)(ty + row * 272 + col * 2) = pk;
        }
    }
    __syncthreads();
    #pragma unroll
    for (int it = 0; it < 8; ++it) {
        int e = it * 512 + tid, row = e >> 4, c = e & 15;
        if (row < nvalid) {
            uint4 v = *(const uint4*)(ty + row * 272 + c * 16);
            *(uint4*)(out + ((size_t)b * CL + l0 + row) * CD + n0 + c * 8) = v;
        }
    }
}

extern "C" void kernel_launch(void* const* d_in, const int* in_sizes, int n_in,
                              void* d_out, int out_size, void* d_ws, size_t ws_size,
                              hipStream_t stream) {
    const unsigned short* x = (const unsigned short*)d_in[0];
    const unsigned short* w = (const unsigned short*)d_in[1];
    const unsigned short* bias = (const unsigned short*)d_in[2];
    const unsigned short* gamma = (const unsigned short*)d_in[3];
    const unsigned short* beta = (const unsigned short*)d_in[4];
    unsigned short* out = (unsigned short*)d_out;
    char* ws = (char*)d_ws;
    unsigned* cnt = (unsigned*)ws;
    float* part = (float*)(ws + (20u << 20));
    hipMemsetAsync(cnt, 0, 1024, stream);
    k_gemm<<<256, 512, 0, stream>>>(x, w, bias, gamma, beta, out, cnt, part);
}
```

```cpp
#include <hip/hip_runtime.h>
#include <hip/hip_bf16.h>
#include <stdint.h>

using bf16x8 = __attribute__((ext_vector_type(8))) short;
using s16x4  = __attribute__((ext_vector_type(4))) short;
using f32x4  = __attribute__((ext_vector_type(4))) float;
typedef __attribute__((address_space(3))) s16x4* lds_s16x4_ptr;

static constexpr int CB = 2, CT = 16000, CD = 512, CL = 7998;
static constexpr int BM = 256, BN = 128;
static constexpr int MT_PER_B = 32;
static constexpr int A_PLANE_BYTES = 272 * 64;
static constexpr int A_BYTES = 2 * A_PLANE_BYTES;
static constexpr int B_BYTES = 5 * 8192;
static constexpr int STAGE_BYTES = A_BYTES + B_BYTES;
static constexpr int NSTAGE = 16;

__device__ __forceinline__ float bf2f(unsigned short u) { return __uint_as_float(((unsigned)u) << 16); }
__device__ __forceinline__ unsigned short f2bf(float f) {
    __hip_bfloat16 h = __float2bfloat16(f);
    return *reinterpret_cast<unsigned short*>(&h);
}

__device__ __forceinline__ float gelu_tanh(float v) {
    const float k1 = -2.0f * 0.7978845608028654f * 1.4426950408889634f;
    const float k2 = k1 * 0.044715f;
    float arg = v * __builtin_fmaf(v * v, k2, k1);
    float e = __builtin_amdgcn_exp2f(arg);
    return v * __builtin_amdgcn_rcpf(1.0f + e);
}
typedef __attribute__((ext_vector_type(2))) __bf16 bf16x2_t;
typedef __attribute__((ext_vector_type(2))) float f32x2_t;
__device__ __forceinline__ unsigned pk_bf16(float a, float b) {
    f32x2_t f = {a, b};
    bf16x2_t h = __builtin_convertvector(f, bf16x2_t);
    return __builtin_bit_cast(unsigned, h);
}

__global__ __launch_bounds__(512) void k_gemm(const unsigned short* __restrict__ x, const unsigned short* __restrict__ w,
                                              const unsigned short* __restrict__ bias, const unsigned short* __restrict__ gamma,
                                              const unsigned short* __restrict__ beta, unsigned short* __restrict__ out,
                                              unsigned* __restrict__ cnt  , float* __restrict__ part  ) {
    __shared__ __attribute__((aligned(1024))) char smem[2 * STAGE_BYTES + 4096 + 512 + 256];
    const int bid = blockIdx.x;
    const int xcd = bid & 7, idx = bid >> 3;
    const int mtg = xcd * 8 + (idx >> 2), nt = idx & 3;
    const int b = mtg >> 5, mt = mtg & 31;
    const int tid = threadIdx.x, wid = tid >> 6, lane = tid & 63;
    const int wr = wid >> 1, wc = wid & 1, fr = lane & 15, fq = lane >> 4;
    const int l0 = mt * BM, n0 = nt * BN;

    unsigned pA[5], pB[5];
    int dA[5], dB[5];
    #pragma unroll
    for (int i = 0; i < 5; ++i) {
        int j = wid + 8 * i; if (j > 33) j = 33;
        int p = j / 17, jb = j % 17;
        int rr = jb * 16 + (lane >> 2);
        int ch = (lane & 3) ^ (((rr >> 2) & 1) << 1);
        int t = 2 * (l0 + rr) + p; if (t > CT - 1) t = CT - 1;
        pA[i] = (unsigned)(((size_t)b * CT + t) * CD + ch * 8) * 2u;
        dA[i] = B_BYTES + p * A_PLANE_BYTES + jb * 1024 + lane * 16;
        int jj = wid + 8 * i; int tap = jj >> 3, kb = jj & 7;
        int k = kb * 4 + (lane >> 4), c16 = lane & 15;
        int h = (k & 3) | (((k >> 3) & 1) << 2);
        int nb = (c16 >> 1) ^ h;
        pB[i] = (unsigned)(((tap * CD + k) * CD) + n0 + nb * 16 + (c16 & 1) * 8) * 2u;
        dB[i] = tap * 8192 + kb * 1024 + lane * 16;
    }
    unsigned sdA[5], sdB[5];
    const unsigned smem_base = (unsigned)(size_t)smem;
    #pragma unroll
    for (int i = 0; i < 5; ++i) {
        sdA[i] = __builtin_amdgcn_readfirstlane(smem_base + dA[i] - lane * 16);
        sdB[i] = __builtin_amdgcn_readfirstlane(smem_base + dB[i] - lane * 16);
    }
    auto glds = [&](unsigned voff, const char* sbase, unsigned dst) {
        unsigned keep;
        asm volatile("s_mov_b32 %0, m0\n\ts_mov_b32 m0, %2\n\ts_nop 0\n\tglobal_load_lds_dwordx4 %1, %3\n\ts_mov_b32 m0, %0"
                     : "=&s"(keep) : "v"(voff), "s"(dst), "s"(sbase) : "memory");
    };
    auto issueA = [&](int i, int s, int buf) { if (i < 4 || wid < 2) glds(pA[i], (const char*)x + s * 64, sdA[i] + buf * STAGE_BYTES); };
    auto issueB = [&](int i, int s, int buf) { glds(pB[i], (const char*)w + (size_t)s * 32 * CD * 2, sdB[i] + buf * STAGE_BYTES); };
    auto issue = [&](int s, int buf) {
        #pragma unroll
        for (int i = 0; i < 5; ++i) issueA(i, s, buf);
        #pragma unroll
        for (int i = 0; i < 5; ++i) issueB(i, s, buf);
    };

    unsigned pfx; { int rr = nt * 129 + wid * 64 + lane; int t = 2 * l0 + rr; if (rr > 514) t = 2 * l0 + 514; if (t > CT - 1) t = CT - 1; pfx = (unsigned)(((size_t)b * CT + t) * CD) * 2u; }
    unsigned pfw; { int e = (mtg & 7) * 40 + (lane < 40 ? lane : 39); int row = e >> 1, hf = e & 1; int tap = row >> 5, k = row & 31; pfw = (unsigned)(((tap * CD + k) * CD) + n0 + hf * 64) * 2u; }
    const unsigned sdummy = __builtin_amdgcn_readfirstlane(smem_base + 2 * STAGE_BYTES + 4096 + 512);
    auto glds4 = [&](unsigned voff, const char* sbase) {
        unsigned keep;
        asm volatile("s_mov_b32 %0, m0\n\ts_mov_b32 m0, %2\n\ts_nop 0\n\tglobal_load_lds_dword %1, %3\n\ts_mov_b32 m0, %0"
                     : "=&s"(keep) : "v"(voff), "s"(sdummy), "s"(sbase) : "memory");
    };
    const int swid = __builtin_amdgcn_readfirstlane(wid);
    auto prefX = [&](int j) -> bool { if (swid < 3 && j < 8) { glds4(pfx, (const char*)x + j * 128); return true; } return false; };
    auto prefW = [&](int sw) -> bool { if (swid == 3 && sw < NSTAGE) { glds4(pfw, (const char*)w + (size_t)sw * 32 * CD * 2); return true; } return false; };

    f32x4 acc[4][4] = {};
    const int g = lane >> 4, q = (lane & 15) >> 2, pp = lane & 3;

    unsigned rB[4], rA[3];
    {
        int k = 8 * g + q; int h = (k & 3) | (((k >> 3) & 1) << 2);
        #pragma unroll
        for (int n = 0; n < 4; ++n) rB[n] = smem_base + k * 256 + (((wc * 4 + n) ^ h) * 32) + pp * 8;
        #pragma unroll
        for (int sft = 0; sft < 3; ++sft) {
            int row = wr * 64 + fr + sft;
            rA[sft] = smem_base + B_BYTES + row * 64 + ((fq ^ (((row >> 2) & 1) << 1)) * 16);
        }
    }
    typedef __attribute__((address_space(3))) const bf16x8* lds_bf16x8_ptr;
    auto ldfrag = [&](unsigned boff, int tap, bf16x8 (&af)[4], bf16x8 (&bfr)[4]) {
        #pragma unroll
        for (int n = 0; n < 4; ++n) {
            unsigned ad = rB[n] + boff;
            s16x4 lo = __builtin_amdgcn_ds_read_tr16_b64_v4i16((lds_s16x4_ptr)(ad + tap * 8192));
            s16x4 hi = __builtin_amdgcn_ds_read_tr16_b64_v4i16((lds_s16x4_ptr)(ad + tap * 8192 + 1024));
            bfr[n] = __builtin_shufflevector(lo, hi, 0, 1, 2, 3, 4, 5, 6, 7);
        }
        unsigned aa = rA[tap >> 1] + boff;
        #pragma unroll
        for (int m = 0; m < 4; ++m)
            af[m] = *(lds_bf16x8_ptr)(aa + (tap & 1) * A_PLANE_BYTES + m * 1024);
    };
    auto mma = [&](bf16x8 (&af)[4], bf16x8 (&bfr)[4]) {
        __builtin_amdgcn_s_setprio(1);
        #pragma unroll
        for (int n = 0; n < 4; ++n)
            #pragma unroll
            for (int m = 0; m < 4; ++m)
                acc[n][m] = __builtin_amdgcn_mfma_f32_16x16x32_bf16(bfr[n], af[m], acc[n][m], 0, 0, 0);
        __builtin_amdgcn_s_setprio(0);
    };
    bf16x8 a0[4], b0[4], a1[4], b1[4];
    issue(0, 0);
    if (tid < 128) ((float*)(smem + 2 * STAGE_BYTES + 4096))[tid] = bf2f(bias[n0 + tid]);
    asm volatile("s_waitcnt vmcnt(0)" ::: "memory");
    prefX(1); prefW(1); prefW(2);
    __syncthreads();
    ldfrag(0u, 0, a0, b0);
    #pragma unroll 1
    for (int s = 0; s < NSTAGE; s += 2) {
        {
            unsigned base = 0u; asm volatile("" : "+s"(base));
            unsigned nbase = STAGE_BYTES; asm volatile("" : "+s"(nbase));
            issueA(0, s + 1, 1); issueB(0, s + 1, 1); issueA(1, s + 1, 1); issueB(1, s + 1, 1);
            ldfrag(base, 1, a1, b1); mma(a0, b0);
            issueA(2, s + 1, 1); issueB(2, s + 1, 1); issueA(3, s + 1, 1); issueB(3, s + 1, 1);
            ldfrag(base, 2, a0, b0); mma(a1, b1);
            issueA(4, s + 1, 1); issueB(4, s + 1, 1);
            bool pf = prefX((s >> 1) + 2); pf = prefW(s + 3) || pf;
            ldfrag(base, 3, a1, b1); mma(a0, b0);
            ldfrag(base, 4, a0, b0); mma(a1, b1);
            if (pf) asm volatile("s_waitcnt vmcnt(1)" ::: "memory"); else asm volatile("s_waitcnt vmcnt(0)" ::: "memory");
            __syncthreads();
            ldfrag(nbase, 0, a1, b1); mma(a0, b0);
        }
        {
            unsigned base = STAGE_BYTES; asm volatile("" : "+s"(base));
            unsigned nbase = 0u; asm volatile("" : "+s"(nbase));
            const bool more = s + 2 < NSTAGE;
            if (more) { issueA(0, s + 2, 0); issueB(0, s + 2, 0); issueA(1, s + 2, 0); issueB(1, s + 2, 0); }
            ldfrag(base, 1, a0, b0); mma(a1, b1);
            if (more) { issueA(2, s + 2, 0); issueB(2, s + 2, 0); issueA(3, s + 2, 0); issueB(3, s + 2, 0); }
            ldfrag(base, 2, a1, b1); mma(a0, b0);
            if (more) { issueA(4, s + 2, 0); issueB(4, s + 2, 0); }
            bool pf = prefW(s + 4);
            ldfrag(base, 3, a0, b0); mma(a1, b1);
            ldfrag(base, 4, a1, b1); mma(a0, b0);
            if (pf) asm volatile("s_waitcnt vmcnt(1)" ::: "memory"); else asm volatile("s_waitcnt vmcnt(0)" ::: "memory");
            __syncthreads();
            if (more) ldfrag(nbase, 0, a0, b0);
            mma(a1, b1);
        }
    }
    asm volatile("s_waitcnt vmcnt(0)" ::: "memory");
    __syncthreads();
    int nvalid = CL - l0; if (nvalid > BM) nvalid = BM;
    int wvalid = nvalid - wr * 64; wvalid = wvalid < 0 ? 0 : (wvalid > 64 ? 64 : wvalid);
    const float winv = wvalid > 0 ? 1.0f / (float)wvalid : 0.f;
    float* wst = (float*)(smem + 2 * STAGE_BYTES);
    const float* sbias = (const float*)(smem + 2 * STAGE_BYTES + 4096);
    if (nvalid == BM)
    {
        float sm[16];
        #pragma unroll
        for (int nf = 0; nf < 4; ++nf) {
            f32x4 bv = *(const f32x4*)(sbias + wc * 64 + nf * 16 + 4 * fq);
            #pragma unroll
            for (int r = 0; r < 4; ++r) {
                float s = 0.f;
                #pragma unroll
                for (int mf = 0; mf < 4; ++mf) {
                    float v = acc[nf][mf][r] + bv[r]; acc[nf][mf][r] = v;
                    s += v;
                }
                sm[nf * 4 + r] = s;
            }
        }
        #define DPP_STEP_F(arr, ctrl) _Pragma("unroll") for (int i = 0; i < 16; ++i) arr[i] += __builtin_bit_cast(float, __builtin_amdgcn_update_dpp(0, __builtin_bit_cast(int, arr[i]), ctrl, 0xf, 0xf, false));
        DPP_STEP_F(sm, 0x128) DPP_STEP_F(sm, 0x124) DPP_STEP_F(sm, 0x122) DPP_STEP_F(sm, 0x121)
        float s2[16];
        #pragma unroll
        for (int nf = 0; nf < 4; ++nf)
            #pragma unroll
            for (int r = 0; r < 4; ++r) {
                float mu = sm[nf * 4 + r] * winv; sm[nf * 4 + r] = mu;
                float s = 0.f;
                #pragma unroll
                for (int mf = 0; mf < 4; ++mf) {
                    float d = acc[nf][mf][r] - mu;
                    s = __builtin_fmaf(d, d, s);
                }
                s2[nf * 4 + r] = s;
            }
        DPP_STEP_F(s2, 0x128) DPP_STEP_F(s2, 0x124) DPP_STEP_F(s2, 0x122) DPP_STEP_F(s2, 0x121)
        if (fr == 0) {
            #pragma unroll
            for (int nf = 0; nf < 4; ++nf)
                #pragma unroll
                for (int r = 0; r < 4; ++r) { float2 o; o.x = sm[nf * 4 + r]; o.y = s2[nf * 4 + r]; *(float2*)(wst + (wr * 128 + wc * 64 + nf * 16 + 4 * fq + r) * 2) = o; }
        }
    }
    else
    {
        float sm[16];
        #pragma unroll
        for (int nf = 0; nf < 4; ++nf) {
            f32x4 bv = *(const f32x4*)(sbias + wc * 64 + nf * 16 + 4 * fq);
            #pragma unroll
            for (int r = 0; r < 4; ++r) {
                float s = 0.f;
                #pragma unroll
                for (int mf = 0; mf < 4; ++mf) {
                    float v = acc[nf][mf][r] + bv[r]; acc[nf][mf][r] = v;
                    s += (mf * 16 + fr < wvalid) ? v : 0.f;
                }
                sm[nf * 4 + r] = s;
            }
        }
        #define DPP_STEP(arr, ctrl) _Pragma("unroll") for (int i = 0; i < 16; ++i) arr[i] += __builtin_bit_cast(float, __builtin_amdgcn_update_dpp(0, __builtin_bit_cast(int, arr[i]), ctrl, 0xf, 0xf, false));
        DPP_STEP(sm, 0x128) DPP_STEP(sm, 0x124) DPP_STEP(sm, 0x122) DPP_STEP(sm, 0x121)
        float s2[16];
        #pragma unroll
        for (int nf = 0; nf < 4; ++nf)
            #pragma unroll
            for (int r = 0; r < 4; ++r) {
                float mu = sm[nf * 4 + r] * winv; sm[nf * 4 + r] = mu;
                float s = 0.f;
                #pragma unroll
                for (int mf = 0; mf < 4; ++mf) {
                    float d = acc[nf][mf][r] - mu;
                    d = (mf * 16 + fr < wvalid) ? d : 0.f;
                    s = __builtin_fmaf(d, d, s);
                }
                s2[nf * 4 + r] = s;
            }
        DPP_STEP(s2, 0x128) DPP_STEP(s2, 0x124) DPP_STEP(s2, 0x122) DPP_STEP(s2, 0x121)
        if (fr == 0) {
            #pragma unroll
            for (int nf = 0; nf < 4; ++nf)
                #pragma unroll
                for (int r = 0; r < 4; ++r) { float2 o; o.x = sm[nf * 4 + r]; o.y = s2[nf * 4 + r]; *(float2*)(wst + (wr * 128 + wc * 64 + nf * 16 + 4 * fq + r) * 2) = o; }
        }
    }
    __syncthreads();
    const int grp = b * 4 + nt;
    if (tid < 128) {
        float n = 0.f, mean = 0.f, M2 = 0.f;
        if (nvalid == BM) {
            float2 p0 = *(const float2*)(wst + (0 * 128 + tid) * 2), p1 = *(const float2*)(wst + (1 * 128 + tid) * 2);
            float2 p2 = *(const float2*)(wst + (2 * 128 + tid) * 2), p3 = *(const float2*)(wst + (3 * 128 + tid) * 2);
            mean = 0.25f * ((p0.x + p1.x) + (p2.x + p3.x));
            float d0 = p0.x - mean, d1 = p1.x - mean, d2 = p2.x - mean, d3 = p3.x - mean;
            M2 = (p0.y + p1.y) + (p2.y + p3.y) + 64.0f * ((d0 * d0 + d1 * d1) + (d2 * d2 + d3 * d3));
        } else
        #pragma unroll
        for (int q4 = 0; q4 < 4; ++q4) {
            int nb_i = nvalid - q4 * 64; nb_i = nb_i < 0 ? 0 : (nb_i > 64 ? 64 : nb_i);
            if (nb_i > 0) {
                float2 p = *(const float2*)(wst + (q4 * 128 + tid) * 2);
                float nb = (float)nb_i, nn = n + nb, delta = p.x - mean;
                mean += delta * nb / nn;
                M2 += p.y + delta * delta * n * nb / nn;
                n = nn;
            }
        }
        unsigned long long bits = ((unsigned long long)__float_as_uint(M2) << 32) | (unsigned long long)__float_as_uint(mean);
        if (bits == ~0ull) bits = 0x7FC000007FC00000ull;
        __hip_atomic_store((unsigned long long*)(part + (((size_t)grp * MT_PER_B + mt) * 128 + tid) * 2), bits, __ATOMIC_RELAXED, __HIP_MEMORY_SCOPE_AGENT);
    }
    char* ty = smem;
    #pragma unroll
    for (int nf = 0; nf < 4; ++nf)
        #pragma unroll
        for (int mf = 0; mf < 4; ++mf) {
            int row = wr * 64 + mf * 16 + fr, col = wc * 64 + nf * 16 + 4 * fq;
            *(f32x4*)(ty + row * 528 + col * 4) = acc[nf][mf];
        }
    float* ssc = wst + 1024 - 256;
    float* ssh = wst + 1024 - 128;
    float fsc = 0.f, fsh = 0.f;
    if (tid < 128) {
        unsigned long long pb[MT_PER_B];
        const unsigned long long* pp0 = (const unsigned long long*)(part + ((size_t)grp * MT_PER_B * 128 + tid) * 2);
        unsigned spins = 0;
        while (true) {
            bool ok = true;
            #pragma unroll
            for (int t = 0; t < MT_PER_B; ++t) { pb[t] = __hip_atomic_load(pp0 + t * 128, __ATOMIC_RELAXED, __HIP_MEMORY_SCOPE_AGENT); ok = ok && (pb[t] != ~0ull); }
            if (__all(ok)) break;
            if (++spins > 200000u) break;
            __builtin_amdgcn_s_sleep(1);
        }
        float ms = 0.f;
        #pragma unroll
        for (int t = 0; t < MT_PER_B; ++t) { float nb = (float)((CL - t * BM) > BM ? BM : (CL - t * BM)); ms += nb * __uint_as_float((unsigned)pb[t]); }
        float mean = ms * (1.0f / (float)CL);
        float M2 = 0.f;
        #pragma unroll
        for (int t = 0; t < MT_PER_B; ++t) { float nb = (float)((CL - t * BM) > BM ? BM : (CL - t * BM)); float dl = __uint_as_float((unsigned)pb[t]) - mean; M2 += __uint_as_float((unsigned)(pb[t] >> 32)) + nb * dl * dl; }
        float rs = rsqrtf(M2 * (1.0f / (float)CL) + 1e-5f);
        float gm = bf2f(gamma[n0 + tid]), be = bf2f(beta[n0 + tid]);
        fsc = rs * gm; fsh = be - mean * rs * gm;
    }
    __syncthreads();
    if (tid < 128) { ssc[tid] = fsc; ssh[tid] = fsh; }
    __syncthreads();
    {
        const int c = tid & 15;
        f32x4 sa = *(const f32x4*)(ssc + c * 8), sb = *(const f32x4*)(ssc + c * 8 + 4);
        f32x4 ha = *(const f32x4*)(ssh + c * 8), hb = *(const f32x4*)(ssh + c * 8 + 4);
        #pragma unroll
        for (int it = 0; it < 8; ++it) {
            int row = it * 32 + (tid >> 4);
            if (row < nvalid) {
                f32x4 va = *(const f32x4*)(ty + row * 528 + c * 32);
                f32x4 vb = *(const f32x4*)(ty + row * 528 + c * 32 + 16);
                uint4 o;
                o.x = pk_bf16(gelu_tanh(__builtin_fmaf(va[0], sa[0], ha[0])), gelu_tanh(__builtin_fmaf(va[1], sa[1], ha[1])));
                o.y = pk_bf16(gelu_tanh(__builtin_fmaf(va[2], sa[2], ha[2])), gelu_tanh(__builtin_fmaf(va[3], sa[3], ha[3])));
                o.z = pk_bf16(gelu_tanh(__builtin_fmaf(vb[0], sb[0], hb[0])), gelu_tanh(__builtin_fmaf(vb[1], sb[1], hb[1])));
                o.w = pk_bf16(gelu_tanh(__builtin_fmaf(vb[2], sb[2], hb[2])), gelu_tanh(__builtin_fmaf(vb[3], sb[3], hb[3])));
                *(uint4*)(out + ((size_t)b * CL + l0 + row) * CD + n0 + c * 8) = o;
            }
        }
    }
}

__global__ void k_init(uint4* __restrict__ p) { p[blockIdx.x * 256 + threadIdx.x] = make_uint4(~0u, ~0u, ~0u, ~0u); }

extern "C" void kernel_launch(void* const* d_in, const int* in_sizes, int n_in,
                              void* d_out, int out_size, void* d_ws, size_t ws_size,
                              hipStream_t stream) {
    const unsigned short* x = (const unsigned short*)d_in[0];
    const unsigned short* w = (const unsigned short*)d_in[1];
    const unsigned short* bias = (const unsigned short*)d_in[2];
    const unsigned short* gamma = (const unsigned short*)d_in[3];
    const unsigned short* beta = (const unsigned short*)d_in[4];
    unsigned short* out = (unsigned short*)d_out;
    char* ws = (char*)d_ws;
    unsigned* cnt = (unsigned*)ws;
    float* part = (float*)(ws + (20u << 20));
    k_init<<<64, 256, 0, stream>>>((uint4*)part);
    k_gemm<<<256, 512, 0, stream>>>(x, w, bias, gamma, beta, out, cnt, part);
}
```
